```python
import torch

def _softmax(x, axis=-1):
    return torch.softmax(x, dim=axis)
def _gelu(x):
    return torch.nn.functional.gelu(x, approximate="tanh")
def _silu(x):
    return torch.nn.functional.silu(x)
def _sigmoid(x):
    return torch.sigmoid(x)
def _layernorm(x, axis=-1, eps=1e-5):
    m = x.mean(dim=axis, keepdim=True)
    v = x.var(dim=axis, keepdim=True, unbiased=False)
    return (x - m) / torch.sqrt(v + eps)
def _scatter_add(tgt, idx, src, axis=0):
    return tgt.clone().index_add_(axis, idx, src)
def _segment_sum(src, seg, n):
    out = torch.zeros((n,) + src.shape[1:], dtype=src.dtype)
    return out.index_add_(0, seg, src)
def _pad(x, pw):
    flat = tuple(v for pair in reversed(pw) for v in pair)
    return torch.nn.functional.pad(x, flat)

kernel_name = 'standalone__kv_cache_quant_int4_int2_kv_with_per_channel_or_per_token_scales__decode'

def setup_inputs():

    _w = (lambda a: (lambda t: t.to(torch.bfloat16) if t.is_floating_point() else t)(torch.as_tensor(a)))
    B = 1
    Sq = 1
    Skv = 65536
    Hq = 32
    Hkv = 16
    Dh = 128
    bits = 4
    G = -1
    if G <= 0 or G >= Skv:
        G = max(1, Skv // 2)
    Skv = (Skv // G) * G
    ngk = Skv // G
    qmax = (1 << bits) - 1
    return {
        "q": _w((lambda *s: torch.randn(*s, dtype=torch.bfloat16) * 1.6)(B, Hq, Sq, Dh)),
        "qk": _w(torch.randint(0, qmax + 1, (B, Hkv, Skv, Dh)).to(torch.int32)),
        "qv": _w(torch.randint(0, qmax + 1, (B, Hkv, Skv, Dh)).to(torch.int32)),
        "k_scales": _w(torch.abs((lambda *s: torch.randn(*s, dtype=torch.bfloat16) * 1.6)(B, Hkv, ngk, Dh)) * 0.05 + 0.02),
        "k_zeros": _w((lambda *s: torch.randn(*s, dtype=torch.bfloat16) * 1.6)(B, Hkv, ngk, Dh) + float(qmax) / 2.0),
        "v_scales": _w(torch.abs((lambda *s: torch.randn(*s, dtype=torch.bfloat16) * 0.05)(B, Hkv, Skv, 1)) * 0.05 + 0.02),
        "v_zeros": _w((lambda *s: torch.randn(*s, dtype=torch.bfloat16) * 0.05)(B, Hkv, Skv, 1) + float(qmax) / 2.0),
    }

def reference(q, qk, qv, k_scales, k_zeros, v_scales, v_zeros):
    B, Hq, Sq, Dh = q.shape
    Hkv, Skv = qk.shape[1], qk.shape[2]
    ngk = k_scales.shape[2]
    G = Skv // ngk
    ks = (lambda x, n, axis: torch.repeat_interleave(x, n, dim=axis))(k_scales, G, axis=2)
    kz = (lambda x, n, axis: torch.repeat_interleave(x, n, dim=axis))(k_zeros, G, axis=2)
    k = ((lambda x, dt: x.to(dt))(qk, q.dtype) - kz) * ks
    v = ((lambda x, dt: x.to(dt))(qv, q.dtype) - v_zeros) * v_scales
    rep = Hq // Hkv
    if rep > 1:
        k = (lambda x, n, axis: torch.repeat_interleave(x, n, dim=axis))(k, rep, axis=1)
        v = (lambda x, n, axis: torch.repeat_interleave(x, n, dim=axis))(v, rep, axis=1)
    scale = float(Dh) ** -0.5
    logits = torch.einsum("bhqd,bhkd->bhqk", q, k) * scale
    p = _softmax(logits, axis=-1)
    o = torch.einsum("bhqk,bhkd->bhqd", p, v)
    return o
```

```cpp
#include <hip/hip_runtime.h>
#include <hip/hip_bf16.h>

#define KV_WAVES 4
#define KV_GPW 64
#define KV_DEPTH 4
#define KV_AUX 2
#define KV_MINW 2

namespace kvq {

constexpr int HQ = 32, HKV = 16, DH = 128, SKV = 65536, NGK = 2, GTOK = SKV / NGK;
constexpr int WAVES = KV_WAVES, NTHR = WAVES * 64, GPW = KV_GPW, DEPTH = KV_DEPTH;
constexpr int TPW = GPW * 8;
constexpr int TPB = TPW * WAVES;
constexpr int NSPLIT = SKV / TPB;
static_assert(SKV % TPB == 0 && GTOK % TPB == 0, "split must stay inside one K quant group");
static_assert(GPW % DEPTH == 0, "");

typedef int   i32x4 __attribute__((ext_vector_type(4)));
typedef unsigned u32x2 __attribute__((ext_vector_type(2)));
typedef unsigned u32x4 __attribute__((ext_vector_type(4)));
typedef float f32x2 __attribute__((ext_vector_type(2)));
typedef float f32x4 __attribute__((ext_vector_type(4)));

__device__ __forceinline__ float bf_lo(unsigned u) { return __builtin_bit_cast(float, u << 16); }
__device__ __forceinline__ float bf_hi(unsigned u) { return __builtin_bit_cast(float, u & 0xffff0000u); }

template <int CTRL> __device__ __forceinline__ float dpp(float x) {
  return __builtin_bit_cast(float, __builtin_amdgcn_mov_dpp(__builtin_bit_cast(int, x), CTRL, 0xf, 0xf, true));
}
constexpr int XOR1 = 0xB1, XOR2 = 0x4E, XOR7 = 0x141, XOR8 = 0x128;

__device__ __forceinline__ float x16_max(float x) {
  auto s = __builtin_amdgcn_permlane16_swap(__float_as_uint(x), __float_as_uint(x), false, false);
  x = fmaxf(__uint_as_float(s[0]), __uint_as_float(s[1]));
  auto t = __builtin_amdgcn_permlane32_swap(__float_as_uint(x), __float_as_uint(x), false, false);
  return fmaxf(__uint_as_float(t[0]), __uint_as_float(t[1]));
}
__device__ __forceinline__ float x16_sum(float x) {
  auto s = __builtin_amdgcn_permlane16_swap(__float_as_uint(x), __float_as_uint(x), false, false);
  x = __uint_as_float(s[0]) + __uint_as_float(s[1]);
  auto t = __builtin_amdgcn_permlane32_swap(__float_as_uint(x), __float_as_uint(x), false, false);
  return __uint_as_float(t[0]) + __uint_as_float(t[1]);
}

constexpr size_t WS_PML = 0, WS_PO = 65536;
static_assert((size_t)HQ * NSPLIT * 2 * 4 <= WS_PO, "");

__global__ void __launch_bounds__(NTHR, KV_MINW)
kv_split(const unsigned short* __restrict__ q, const int* __restrict__ qk, const int* __restrict__ qv,
         const unsigned short* __restrict__ ks, const unsigned short* __restrict__ kz,
         const unsigned short* __restrict__ vs, const unsigned short* __restrict__ vz,
         float* __restrict__ pml, float* __restrict__ po) {
  __shared__ __attribute__((aligned(16))) unsigned lds_sz[WAVES][TPW];
  __shared__ __attribute__((aligned(16))) float lds_acc[WAVES][2][DH];
  __shared__ float lds_ml[WAVES][2][4];

  const int split = blockIdx.x, kvh = blockIdx.y;
  const int tid = threadIdx.x, lane = tid & 63;
  const int wave = __builtin_amdgcn_readfirstlane(tid >> 6);
  const int s = lane & 7, r = lane >> 3;
  const int t0 = split * TPB + wave * TPW;
  const int grp = t0 / GTOK;

  u32x2 qa[2][4], ksa[4], kza[4];
  const unsigned short* qp = q + (size_t)(kvh * 2) * DH + 4 * s;
  const unsigned short* ksp = ks + (size_t)(kvh * NGK + grp) * DH + 4 * s;
  const unsigned short* kzp = kz + (size_t)(kvh * NGK + grp) * DH + 4 * s;
#pragma unroll
  for (int j = 0; j < 4; ++j) {
    qa[0][j] = *reinterpret_cast<const u32x2*>(qp + 32 * j);
    qa[1][j] = *reinterpret_cast<const u32x2*>(qp + DH + 32 * j);
    ksa[j] = *reinterpret_cast<const u32x2*>(ksp + 32 * j);
    kza[j] = *reinterpret_cast<const u32x2*>(kzp + 32 * j);
  }
  constexpr int SZL = TPW / 64;
  static_assert(SZL == 2 || SZL == 4 || SZL == 8, "");
  unsigned vsr[SZL / 2], vzr[SZL / 2];
  {
    const unsigned* vsp = reinterpret_cast<const unsigned*>(vs + (size_t)kvh * SKV + t0 + lane * SZL);
    const unsigned* vzp = reinterpret_cast<const unsigned*>(vz + (size_t)kvh * SKV + t0 + lane * SZL);
#pragma unroll
    for (int i = 0; i < SZL / 2; ++i) { vsr[i] = vsp[i]; vzr[i] = vzp[i]; }
  }

  const size_t base_off = ((size_t)kvh * SKV + t0) * DH;
  const __amdgpu_buffer_rsrc_t rk = __builtin_amdgcn_make_buffer_rsrc((void*)(qk + base_off), 0, TPW * DH * 4, 0x00020000);
  const __amdgpu_buffer_rsrc_t rv = __builtin_amdgcn_make_buffer_rsrc((void*)(qv + base_off), 0, TPW * DH * 4, 0x00020000);
  const int voff = r * 512 + s * 16;
  i32x4 kb[DEPTH][4], vb[DEPTH][4];
#define LOADG(buf, rs, g) do { _Pragma("unroll") for (int j_ = 0; j_ < 4; ++j_) \
    buf[j_] = __builtin_bit_cast(i32x4, __builtin_amdgcn_raw_buffer_load_b128(rs, voff + j_ * 128, (g) * 4096, KV_AUX)); } while (0)
#pragma unroll
  for (int d = 0; d < DEPTH; ++d) { LOADG(kb[d], rk, d); LOADG(vb[d], rv, d); }

  constexpr float C2 = 0.088388347648318440f * 1.4426950408889634f;
  f32x2 a0[8], a1[8];
  f32x2 c0 = {0.f, 0.f}, c1 = {0.f, 0.f};
#pragma unroll
  for (int j = 0; j < 4; ++j) {
#pragma unroll
    for (int i = 0; i < 2; ++i) {
      const unsigned q0 = qa[0][j][i], q1 = qa[1][j][i], k_s = ksa[j][i], k_z = kza[j][i];
      const f32x2 fs = f32x2{bf_lo(k_s), bf_hi(k_s)} * C2, fz = {bf_lo(k_z), bf_hi(k_z)};
      const f32x2 w0 = f32x2{bf_lo(q0), bf_hi(q0)} * fs, w1 = f32x2{bf_lo(q1), bf_hi(q1)} * fs;
      a0[j * 2 + i] = w0; a1[j * 2 + i] = w1;
      c0 -= w0 * fz; c1 -= w1 * fz;
    }
  }
#pragma unroll
  for (int i = 0; i < SZL / 2; ++i) {
    lds_sz[wave][lane * SZL + 2 * i]     = (vsr[i] & 0xffffu) | (vzr[i] << 16);
    lds_sz[wave][lane * SZL + 2 * i + 1] = (vsr[i] >> 16) | (vzr[i] & 0xffff0000u);
  }
  __builtin_amdgcn_wave_barrier();

  float m0 = -INFINITY, m1 = -INFINITY, l0 = 0.f, l1 = 0.f, z0 = 0.f, z1 = 0.f;
  f32x2 acc0[8], acc1[8];
#pragma unroll
  for (int i = 0; i < 8; ++i) { acc0[i] = f32x2{0.f, 0.f}; acc1[i] = f32x2{0.f, 0.f}; }

  for (int g0 = 0; g0 < GPW; g0 += DEPTH) {
#pragma unroll
    for (int d = 0; d < DEPTH; ++d) {
      const int g = g0 + d;
      const unsigned sz = lds_sz[wave][g * 8 + r];
      f32x2 s0 = c0, s1 = c1;
#pragma unroll
      for (int j = 0; j < 4; ++j) {
        const f32x2 xa = {(float)kb[d][j][0], (float)kb[d][j][1]}, xb = {(float)kb[d][j][2], (float)kb[d][j][3]};
        s0 += a0[j * 2] * xa; s1 += a1[j * 2] * xa;
        s0 += a0[j * 2 + 1] * xb; s1 += a1[j * 2 + 1] * xb;
      }
      __builtin_amdgcn_sched_barrier(0);
      LOADG(kb[d], rk, g + DEPTH);
      __builtin_amdgcn_sched_barrier(0);
      float sc0 = s0.x + s0.y, sc1 = s1.x + s1.y;
      sc0 += dpp<XOR1>(sc0); sc1 += dpp<XOR1>(sc1);
      sc0 += dpp<XOR2>(sc0); sc1 += dpp<XOR2>(sc1);
      sc0 += dpp<XOR7>(sc0); sc1 += dpp<XOR7>(sc1);
      if (__any(sc0 > m0 || sc1 > m1)) {
        float mx = fmaxf(sc0, dpp<XOR8>(sc0)), my = fmaxf(sc1, dpp<XOR8>(sc1));
        mx = fmaxf(x16_max(mx), m0); my = fmaxf(x16_max(my), m1);
        const float al0 = __builtin_amdgcn_exp2f(m0 - mx), al1 = __builtin_amdgcn_exp2f(m1 - my);
        m0 = mx; m1 = my;
        l0 *= al0; z0 *= al0; l1 *= al1; z1 *= al1;
#pragma unroll
        for (int i = 0; i < 8; ++i) { acc0[i] *= al0; acc1[i] *= al1; }
      }
      const float p0 = __builtin_amdgcn_exp2f(sc0 - m0), p1 = __builtin_amdgcn_exp2f(sc1 - m1);
      l0 += p0; l1 += p1;
      const float w0 = p0 * bf_lo(sz), w1 = p1 * bf_lo(sz);
      z0 = fmaf(w0, bf_hi(sz), z0); z1 = fmaf(w1, bf_hi(sz), z1);
#pragma unroll
      for (int j = 0; j < 4; ++j) {
        const f32x2 xa = {(float)vb[d][j][0], (float)vb[d][j][1]}, xb = {(float)vb[d][j][2], (float)vb[d][j][3]};
        acc0[j * 2] += w0 * xa; acc1[j * 2] += w1 * xa;
        acc0[j * 2 + 1] += w0 * xb; acc1[j * 2 + 1] += w1 * xb;
      }
      __builtin_amdgcn_sched_barrier(0);
      LOADG(vb[d], rv, g + DEPTH);
      __builtin_amdgcn_sched_barrier(0);
    }
  }
#undef LOADG

#pragma unroll
  for (int i = 0; i < 8; ++i) {
    acc0[i] -= z0; acc1[i] -= z1;
#pragma unroll
    for (int e = 0; e < 2; ++e) {
      float x = acc0[i][e], y = acc1[i][e];
      x += dpp<XOR8>(x); y += dpp<XOR8>(y);
      acc0[i][e] = x16_sum(x); acc1[i][e] = x16_sum(y);
    }
  }
  l0 += dpp<XOR8>(l0); l1 += dpp<XOR8>(l1);
  l0 = x16_sum(l0); l1 = x16_sum(l1);
  if (r == 0) {
#pragma unroll
    for (int j = 0; j < 4; ++j) {
      *reinterpret_cast<f32x4*>(&lds_acc[wave][0][32 * j + 4 * s]) = f32x4{acc0[2 * j].x, acc0[2 * j].y, acc0[2 * j + 1].x, acc0[2 * j + 1].y};
      *reinterpret_cast<f32x4*>(&lds_acc[wave][1][32 * j + 4 * s]) = f32x4{acc1[2 * j].x, acc1[2 * j].y, acc1[2 * j + 1].x, acc1[2 * j + 1].y};
    }
    if (s == 0) { lds_ml[wave][0][0] = m0; lds_ml[wave][0][1] = l0; lds_ml[wave][1][0] = m1; lds_ml[wave][1][1] = l1; }
  }
  __syncthreads();
  if (tid < 2 * DH) {
    const int h = tid >> 7, d = tid & 127;
    float M = -INFINITY;
#pragma unroll
    for (int w = 0; w < WAVES; ++w) M = fmaxf(M, lds_ml[w][h][0]);
    float L = 0.f, o = 0.f;
#pragma unroll
    for (int w = 0; w < WAVES; ++w) {
      const float wt = __builtin_amdgcn_exp2f(lds_ml[w][h][0] - M);
      L = fmaf(wt, lds_ml[w][h][1], L);
      o = fmaf(wt, lds_acc[w][h][d], o);
    }
    const size_t slot = (size_t)(kvh * 2 + h) * NSPLIT + split;
    po[slot * DH + d] = o;
    if (d == 0) { pml[slot * 2] = M; pml[slot * 2 + 1] = L; }
  }
}

__global__ void __launch_bounds__(DH) kv_combine(const float* __restrict__ pml, const float* __restrict__ po,
                                                 __hip_bfloat16* __restrict__ out) {
  const int h = blockIdx.x, d = threadIdx.x;
  const float* ml = pml + (size_t)h * NSPLIT * 2;
  const float* pp = po + (size_t)h * NSPLIT * DH + d;
  float ov[NSPLIT];
#pragma unroll
  for (int s = 0; s < NSPLIT; ++s) ov[s] = pp[(size_t)s * DH];
  float M = -INFINITY;
#pragma unroll
  for (int s = 0; s < NSPLIT; ++s) M = fmaxf(M, ml[2 * s]);
  float L = 0.f, o = 0.f;
#pragma unroll
  for (int s = 0; s < NSPLIT; ++s) {
    const float wt = __builtin_amdgcn_exp2f(ml[2 * s] - M);
    L = fmaf(wt, ml[2 * s + 1], L);
    o = fmaf(wt, ov[s], o);
  }
  out[h * DH + d] = __float2bfloat16(o / L);
}

}

extern "C" void kernel_launch(void* const* d_in, const int* in_sizes, int n_in, void* d_out, int out_size,
                              void* d_ws, size_t ws_size, hipStream_t stream) {
  using namespace kvq;
  float* pml = reinterpret_cast<float*>((char*)d_ws + WS_PML);
  float* po = reinterpret_cast<float*>((char*)d_ws + WS_PO);
  kv_split<<<dim3(NSPLIT, HKV), dim3(NTHR), 0, stream>>>(
      (const unsigned short*)d_in[0], (const int*)d_in[1], (const int*)d_in[2], (const unsigned short*)d_in[3],
      (const unsigned short*)d_in[4], (const unsigned short*)d_in[5], (const unsigned short*)d_in[6], pml, po);
  kv_combine<<<dim3(HQ), dim3(DH), 0, stream>>>(pml, po, (__hip_bfloat16*)d_out);
}
```

```cpp
#include <hip/hip_runtime.h>
#include <hip/hip_bf16.h>

#define KV_WAVES 4
#define KV_WGQ 8
#define KV_AUX 2
#define KV_MINW 2
#define KV_SROUNDS 30

namespace kvq {

constexpr int HQ = 32, HKV = 16, DH = 128, SKV = 65536, NGK = 2, GTOK = SKV / NGK;
constexpr int WAVES = KV_WAVES, NTHR = WAVES * 64, WGQ = KV_WGQ;
constexpr int NQ = HKV * NGK;
constexpr int NWG = NQ * WGQ;
constexpr int TT = 32;
constexpr int NT = GTOK / TT;
constexpr int NWQ = WGQ * WAVES;
constexpr int NSPLIT = NGK * WGQ;
constexpr int SBOUND = KV_SROUNDS * NWQ;
static_assert(SBOUND >= 2 * NWQ && SBOUND <= NT, "");

typedef int   i32x4 __attribute__((ext_vector_type(4)));
typedef unsigned u32x2 __attribute__((ext_vector_type(2)));
typedef unsigned u32x4 __attribute__((ext_vector_type(4)));
typedef float f32x2 __attribute__((ext_vector_type(2)));
typedef float f32x4 __attribute__((ext_vector_type(4)));
typedef unsigned long long u64;

__device__ __forceinline__ float bf_lo(unsigned u) { return __builtin_bit_cast(float, u << 16); }
__device__ __forceinline__ float bf_hi(unsigned u) { return __builtin_bit_cast(float, u & 0xffff0000u); }

template <int CTRL> __device__ __forceinline__ float dpp(float x) {
  return __builtin_bit_cast(float, __builtin_amdgcn_mov_dpp(__builtin_bit_cast(int, x), CTRL, 0xf, 0xf, true));
}
constexpr int XOR1 = 0xB1, XOR2 = 0x4E, XOR7 = 0x141, XOR8 = 0x128;

__device__ __forceinline__ float x16_max(float x) {
  auto s = __builtin_amdgcn_permlane16_swap(__float_as_uint(x), __float_as_uint(x), false, false);
  x = fmaxf(__uint_as_float(s[0]), __uint_as_float(s[1]));
  auto t = __builtin_amdgcn_permlane32_swap(__float_as_uint(x), __float_as_uint(x), false, false);
  return fmaxf(__uint_as_float(t[0]), __uint_as_float(t[1]));
}
__device__ __forceinline__ float x16_sum(float x) {
  auto s = __builtin_amdgcn_permlane16_swap(__float_as_uint(x), __float_as_uint(x), false, false);
  x = __uint_as_float(s[0]) + __uint_as_float(s[1]);
  auto t = __builtin_amdgcn_permlane32_swap(__float_as_uint(x), __float_as_uint(x), false, false);
  return __uint_as_float(t[0]) + __uint_as_float(t[1]);
}

constexpr size_t WS_CTL = 0, CTL_STRIDE = 1024, WS_PART = 65536;
constexpr int PREC = 1152;

__device__ __forceinline__ unsigned epoch_tag(unsigned e) { return e * 0x9E3779B1u + 0x7F4A7C15u; }

__device__ __forceinline__ void tag_counter(u64* w, u64 seen, unsigned tag) {
  const u64 want = ((unsigned)(seen >> 32) == tag) ? seen : ((u64)tag << 32);
  u64 expect = seen;
  __hip_atomic_compare_exchange_strong(w, &expect, want, __ATOMIC_RELAXED, __ATOMIC_RELAXED, __HIP_MEMORY_SCOPE_AGENT);
}

__global__ void __launch_bounds__(NTHR, KV_MINW)
kv_decode(const unsigned short* __restrict__ q, const int* __restrict__ qk, const int* __restrict__ qv,
          const unsigned short* __restrict__ ks, const unsigned short* __restrict__ kz,
          const unsigned short* __restrict__ vs, const unsigned short* __restrict__ vz,
          unsigned char* __restrict__ ws, unsigned short* __restrict__ out) {
  __shared__ __attribute__((aligned(16))) float lds_acc[WAVES][2][DH];
  __shared__ __attribute__((aligned(16))) float lds_ml[WAVES][4];

  const int qid = blockIdx.x / WGQ, wgq = blockIdx.x % WGQ;
  const int kvh = qid / NGK, grp = qid % NGK;
  const int tid = threadIdx.x, lane = tid & 63;
  const int wave = __builtin_amdgcn_readfirstlane(tid >> 6);
  const int s = lane & 7, r = lane >> 3;
  const int wq = wgq * WAVES + wave;
  unsigned char* ctl = ws + WS_CTL + (size_t)kvh * CTL_STRIDE;
  unsigned* ep_p = reinterpret_cast<unsigned*>(ctl);
  u64* tq_p = reinterpret_cast<u64*>(ctl + 256 + 256 * grp);
  u64* dn_p = reinterpret_cast<u64*>(ctl + 768);

  const unsigned ep = __hip_atomic_load(ep_p, __ATOMIC_RELAXED, __HIP_MEMORY_SCOPE_AGENT);
  const u64 tq_seen = __hip_atomic_load(tq_p, __ATOMIC_RELAXED, __HIP_MEMORY_SCOPE_AGENT);
  const u64 dn_seen = __hip_atomic_load(dn_p, __ATOMIC_RELAXED, __HIP_MEMORY_SCOPE_AGENT);
  u32x2 qa[2][4], ksa[4], kza[4];
  const unsigned short* qp = q + (size_t)(kvh * 2) * DH + 4 * s;
  const unsigned short* ksp = ks + (size_t)qid * DH + 4 * s;
  const unsigned short* kzp = kz + (size_t)qid * DH + 4 * s;
#pragma unroll
  for (int j = 0; j < 4; ++j) {
    qa[0][j] = *reinterpret_cast<const u32x2*>(qp + 32 * j);
    qa[1][j] = *reinterpret_cast<const u32x2*>(qp + DH + 32 * j);
    ksa[j] = *reinterpret_cast<const u32x2*>(ksp + 32 * j);
    kza[j] = *reinterpret_cast<const u32x2*>(kzp + 32 * j);
  }

  const size_t tok0 = (size_t)qid * GTOK;
  const __amdgpu_buffer_rsrc_t rk = __builtin_amdgcn_make_buffer_rsrc((void*)(qk + tok0 * DH), 0, GTOK * DH * 4, 0x00020000);
  const __amdgpu_buffer_rsrc_t rv = __builtin_amdgcn_make_buffer_rsrc((void*)(qv + tok0 * DH), 0, GTOK * DH * 4, 0x00020000);
  const __amdgpu_buffer_rsrc_t rs = __builtin_amdgcn_make_buffer_rsrc((void*)(vs + tok0), 0, GTOK * 2, 0x00020000);
  const __amdgpu_buffer_rsrc_t rz = __builtin_amdgcn_make_buffer_rsrc((void*)(vz + tok0), 0, GTOK * 2, 0x00020000);
  const __amdgpu_buffer_rsrc_t rc = __builtin_amdgcn_make_buffer_rsrc((void*)tq_p, 0, 4, 0x00020000);
  const int voff_ = r * 2048 + s * 16;
  int voff = voff_;
  i32x4 kb[16], vb[16];
  u32x2 sb, zb;
#define LOADT(buf, rsrc, t) do { _Pragma("unroll") for (int j_ = 0; j_ < 16; ++j_) \
    buf[j_] = __builtin_bit_cast(i32x4, __builtin_amdgcn_raw_buffer_load_b128(rsrc, voff + j_ * 128, (t) * (TT * DH * 4), KV_AUX)); } while (0)
#define LOADSZ(t) do { sb = __builtin_bit_cast(u32x2, __builtin_amdgcn_raw_buffer_load_b64(rs, r * 8, (t) * (TT * 2), KV_AUX)); \
    zb = __builtin_bit_cast(u32x2, __builtin_amdgcn_raw_buffer_load_b64(rz, r * 8, (t) * (TT * 2), KV_AUX)); } while (0)
  const int coff = lane == 0 ? 0 : 64;
  int cur = wq;
  int tkbase = wq + NWQ, tk = 0;
  __builtin_amdgcn_sched_barrier(0);
  LOADT(kb, rk, cur);
  __builtin_amdgcn_sched_barrier(0);
  LOADSZ(cur);
  LOADT(vb, rv, cur);
  __builtin_amdgcn_sched_barrier(0);

  const unsigned tag = epoch_tag(ep);
  if (lane == 0) {
    tag_counter(tq_p, tq_seen, tag);
    if (wave == 0) tag_counter(dn_p, dn_seen, tag);
  }

  constexpr float C2 = 0.088388347648318440f * 1.4426950408889634f;
  f32x2 a0[8], a1[8];
  f32x2 c0 = {0.f, 0.f}, c1 = {0.f, 0.f};
#pragma unroll
  for (int j = 0; j < 4; ++j) {
#pragma unroll
    for (int i = 0; i < 2; ++i) {
      const unsigned q0 = qa[0][j][i], q1 = qa[1][j][i], k_s = ksa[j][i], k_z = kza[j][i];
      const f32x2 fs = f32x2{bf_lo(k_s), bf_hi(k_s)} * C2, fz = {bf_lo(k_z), bf_hi(k_z)};
      const f32x2 w0 = f32x2{bf_lo(q0), bf_hi(q0)} * fs, w1 = f32x2{bf_lo(q1), bf_hi(q1)} * fs;
      a0[j * 2 + i] = w0; a1[j * 2 + i] = w1;
      c0 -= w0 * fz; c1 -= w1 * fz;
    }
  }

  float m0 = -INFINITY, m1 = -INFINITY, l0 = 0.f, l1 = 0.f, z0 = 0.f, z1 = 0.f;
  f32x2 acc0[8], acc1[8];
#pragma unroll
  for (int i = 0; i < 8; ++i) { acc0[i] = f32x2{0.f, 0.f}; acc1[i] = f32x2{0.f, 0.f}; }

  do {
    voff = voff_;
    asm volatile("" : "+v"(voff));
    float sc0[4], sc1[4];
#pragma unroll
    for (int g = 0; g < 4; ++g) {
      f32x2 s0 = c0, s1 = c1;
#pragma unroll
      for (int j = 0; j < 4; ++j) {
        const i32x4 k4 = kb[g * 4 + j];
        const f32x2 xa = {(float)k4[0], (float)k4[1]}, xb = {(float)k4[2], (float)k4[3]};
        s0 += a0[j * 2] * xa; s1 += a1[j * 2] * xa;
        s0 += a0[j * 2 + 1] * xb; s1 += a1[j * 2 + 1] * xb;
      }
      sc0[g] = s0.x + s0.y; sc1[g] = s1.x + s1.y;
    }
#pragma unroll
    for (int g = 0; g < 4; ++g) asm volatile("" : "+v"(sc0[g]), "+v"(sc1[g]));
    __builtin_amdgcn_sched_barrier(0);
    int nxt = tkbase + __builtin_amdgcn_readfirstlane(tk);
    nxt = nxt < NT ? nxt : NT;
    const bool stat = nxt + NWQ < SBOUND;
    tkbase = stat ? nxt + NWQ : SBOUND;
    tk = __builtin_amdgcn_raw_ptr_buffer_atomic_add_i32(1, rc, coff, stat ? 64 : 0, 0);
    LOADT(kb, rk, nxt);
    __builtin_amdgcn_sched_barrier(0);
#pragma unroll
    for (int g = 0; g < 4; ++g) { sc0[g] += dpp<XOR1>(sc0[g]); sc1[g] += dpp<XOR1>(sc1[g]); }
#pragma unroll
    for (int g = 0; g < 4; ++g) { sc0[g] += dpp<XOR2>(sc0[g]); sc1[g] += dpp<XOR2>(sc1[g]); }
#pragma unroll
    for (int g = 0; g < 4; ++g) { sc0[g] += dpp<XOR7>(sc0[g]); sc1[g] += dpp<XOR7>(sc1[g]); }
    const float t0 = fmaxf(fmaxf(sc0[0], sc0[1]), fmaxf(sc0[2], sc0[3]));
    const float t1 = fmaxf(fmaxf(sc1[0], sc1[1]), fmaxf(sc1[2], sc1[3]));
    if (__any(t0 > m0 || t1 > m1)) {
      float mx = fmaxf(t0, dpp<XOR8>(t0)), my = fmaxf(t1, dpp<XOR8>(t1));
      mx = fmaxf(x16_max(mx), m0); my = fmaxf(x16_max(my), m1);
      const float al0 = __builtin_amdgcn_exp2f(m0 - mx), al1 = __builtin_amdgcn_exp2f(m1 - my);
      m0 = mx; m1 = my;
      l0 *= al0; z0 *= al0; l1 *= al1; z1 *= al1;
#pragma unroll
      for (int i = 0; i < 8; ++i) { acc0[i] *= al0; acc1[i] *= al1; }
    }
    float w0[4], w1[4];
#pragma unroll
    for (int g = 0; g < 4; ++g) {
      const float p0 = __builtin_amdgcn_exp2f(sc0[g] - m0), p1 = __builtin_amdgcn_exp2f(sc1[g] - m1);
      l0 += p0; l1 += p1;
      const float fs = (g & 1) ? bf_hi(sb[g >> 1]) : bf_lo(sb[g >> 1]);
      const float fz = (g & 1) ? bf_hi(zb[g >> 1]) : bf_lo(zb[g >> 1]);
      w0[g] = p0 * fs; w1[g] = p1 * fs;
      z0 = fmaf(w0[g], fz, z0); z1 = fmaf(w1[g], fz, z1);
    }
#pragma unroll
    for (int g = 0; g < 4; ++g) {
#pragma unroll
      for (int j = 0; j < 4; ++j) {
        const i32x4 v4 = vb[g * 4 + j];
        const f32x2 xa = {(float)v4[0], (float)v4[1]}, xb = {(float)v4[2], (float)v4[3]};
        acc0[j * 2] += w0[g] * xa; acc1[j * 2] += w1[g] * xa;
        acc0[j * 2 + 1] += w0[g] * xb; acc1[j * 2 + 1] += w1[g] * xb;
      }
    }
#pragma unroll
    for (int i = 0; i < 8; ++i) asm volatile("" : "+v"(acc0[i]), "+v"(acc1[i]));
    asm volatile("" : "+v"(z0), "+v"(z1));
    voff = voff_;
    asm volatile("" : "+v"(voff));
    __builtin_amdgcn_sched_barrier(0);
    LOADSZ(nxt);
    LOADT(vb, rv, nxt);
    __builtin_amdgcn_sched_barrier(0);
    cur = nxt;
  } while (cur < NT);
#undef LOADT
#undef LOADSZ

#pragma unroll
  for (int i = 0; i < 8; ++i) {
    acc0[i] -= z0; acc1[i] -= z1;
#pragma unroll
    for (int e = 0; e < 2; ++e) {
      float x = acc0[i][e], y = acc1[i][e];
      x += dpp<XOR8>(x); y += dpp<XOR8>(y);
      acc0[i][e] = x16_sum(x); acc1[i][e] = x16_sum(y);
    }
  }
  l0 += dpp<XOR8>(l0); l1 += dpp<XOR8>(l1);
  l0 = x16_sum(l0); l1 = x16_sum(l1);
  if (r == 0) {
#pragma unroll
    for (int j = 0; j < 4; ++j) {
      *reinterpret_cast<f32x4*>(&lds_acc[wave][0][32 * j + 4 * s]) = f32x4{acc0[2 * j].x, acc0[2 * j].y, acc0[2 * j + 1].x, acc0[2 * j + 1].y};
      *reinterpret_cast<f32x4*>(&lds_acc[wave][1][32 * j + 4 * s]) = f32x4{acc1[2 * j].x, acc1[2 * j].y, acc1[2 * j + 1].x, acc1[2 * j + 1].y};
    }
    if (s == 0) *reinterpret_cast<f32x4*>(&lds_ml[wave][0]) = f32x4{m0, l0, m1, l1};
  }
  __syncthreads();
  if (wave != 0) return;

  const int h = lane >> 5, d4 = (lane & 31) * 4;
  const __amdgpu_buffer_rsrc_t rp = __builtin_amdgcn_make_buffer_rsrc((void*)(ws + WS_PART + (size_t)kvh * NSPLIT * PREC), 0, NSPLIT * PREC, 0x00020000);
  {
    float M = -INFINITY;
#pragma unroll
    for (int w = 0; w < WAVES; ++w) M = fmaxf(M, lds_ml[w][2 * h]);
    float L = 0.f;
    f32x4 o = {0.f, 0.f, 0.f, 0.f};
#pragma unroll
    for (int w = 0; w < WAVES; ++w) {
      const float wt = __builtin_amdgcn_exp2f(lds_ml[w][2 * h] - M);
      L = fmaf(wt, lds_ml[w][2 * h + 1], L);
      o += wt * *reinterpret_cast<const f32x4*>(&lds_acc[w][h][d4]);
    }
    const int rec = (grp * WGQ + wgq) * PREC;
    __builtin_amdgcn_raw_buffer_store_b128(__builtin_bit_cast(u32x4, o), rp, rec + lane * 16, 0, 16);
    if ((lane & 31) == 0)
      __builtin_amdgcn_raw_buffer_store_b64(__builtin_bit_cast(u32x2, f32x2{M, L}), rp, rec + 1024 + h * 8, 0, 16);
  }
  asm volatile("s_waitcnt vmcnt(0)" ::: "memory");
  unsigned old = 0;
  if (lane == 0) old = __hip_atomic_fetch_add(reinterpret_cast<unsigned*>(dn_p), 1u, __ATOMIC_RELAXED, __HIP_MEMORY_SCOPE_AGENT);
  old = __builtin_amdgcn_readfirstlane(old);
  if (old != NSPLIT - 1) return;
  __builtin_amdgcn_fence(__ATOMIC_ACQUIRE, "wavefront");

  {
    f32x4 ov[NSPLIT];
    f32x2 ml[NSPLIT];
#pragma unroll
    for (int p = 0; p < NSPLIT; ++p) {
      ov[p] = __builtin_bit_cast(f32x4, __builtin_amdgcn_raw_buffer_load_b128(rp, p * PREC + lane * 16, 0, 16));
      ml[p] = __builtin_bit_cast(f32x2, __builtin_amdgcn_raw_buffer_load_b64(rp, p * PREC + 1024 + h * 8, 0, 16));
    }
    float M = -INFINITY;
#pragma unroll
    for (int p = 0; p < NSPLIT; ++p) M = fmaxf(M, ml[p].x);
    float L = 0.f;
    f32x4 o = {0.f, 0.f, 0.f, 0.f};
#pragma unroll
    for (int p = 0; p < NSPLIT; ++p) {
      const float wt = __builtin_amdgcn_exp2f(ml[p].x - M);
      L = fmaf(wt, ml[p].y, L);
      o += wt * ov[p];
    }
    const float inv = 1.0f / L;
    __hip_bfloat16 b[4];
#pragma unroll
    for (int e = 0; e < 4; ++e) b[e] = __float2bfloat16(o[e] * inv);
    u32x2 pk;
    pk[0] = (unsigned)__builtin_bit_cast(unsigned short, b[0]) | ((unsigned)__builtin_bit_cast(unsigned short, b[1]) << 16);
    pk[1] = (unsigned)__builtin_bit_cast(unsigned short, b[2]) | ((unsigned)__builtin_bit_cast(unsigned short, b[3]) << 16);
    *reinterpret_cast<u32x2*>(out + (size_t)(kvh * 2 + h) * DH + d4) = pk;
    if (lane == 0) __hip_atomic_store(ep_p, ep + 1u, __ATOMIC_RELAXED, __HIP_MEMORY_SCOPE_AGENT);
  }
}

}

extern "C" void kernel_launch(void* const* d_in, const int* in_sizes, int n_in, void* d_out, int out_size,
                              void* d_ws, size_t ws_size, hipStream_t stream) {
  using namespace kvq;
  kv_decode<<<dim3(NWG), dim3(NTHR), 0, stream>>>(
      (const unsigned short*)d_in[0], (const int*)d_in[1], (const int*)d_in[2], (const unsigned short*)d_in[3],
      (const unsigned short*)d_in[4], (const unsigned short*)d_in[5], (const unsigned short*)d_in[6],
      (unsigned char*)d_ws, (unsigned short*)d_out);
}
```

```cpp
#include <hip/hip_runtime.h>
#include <hip/hip_bf16.h>

namespace kvq {

constexpr int HKV = 16, DH = 128, SKV = 65536, NGK = 2, GTOK = SKV / NGK;
constexpr int WAVES = 4, NTHR = WAVES * 64, WGQ = 8;
constexpr int NQ = HKV * NGK;
constexpr int NWG = NQ * WGQ;
constexpr int TT = 32;
constexpr int TB = TT * DH * 4;
constexpr int NT = GTOK / TT;
constexpr int NWQ = WGQ * WAVES;
constexpr int S = NT / NWQ;
constexpr int NREC = NGK * WGQ;
constexpr int PACE_T = 214, PACE_0 = 550;
constexpr int BO_LOW = 120, BO_HIGH = 150, BO_SHIFT = 300;
constexpr int AUX_NT = 2, AUX_SC1 = 16;
static_assert(S == 32, "");

typedef int   i32x4 __attribute__((ext_vector_type(4)));
typedef unsigned u32x2 __attribute__((ext_vector_type(2)));
typedef unsigned u32x4 __attribute__((ext_vector_type(4)));
typedef float f32x2 __attribute__((ext_vector_type(2)));
typedef float f32x4 __attribute__((ext_vector_type(4)));
typedef unsigned long long u64;

__device__ __forceinline__ float bf_lo(unsigned u) { return __builtin_bit_cast(float, u << 16); }
__device__ __forceinline__ float bf_hi(unsigned u) { return __builtin_bit_cast(float, u & 0xffff0000u); }

template <int CTRL> __device__ __forceinline__ float dpp(float x) {
  return __builtin_bit_cast(float, __builtin_amdgcn_mov_dpp(__builtin_bit_cast(int, x), CTRL, 0xf, 0xf, true));
}
constexpr int XOR1 = 0xB1, XOR2 = 0x4E, XOR7 = 0x141, XOR8 = 0x128, ROR4 = 0x124;

__device__ __forceinline__ float swap16_add(float a, float b) {
  auto s = __builtin_amdgcn_permlane16_swap(__float_as_uint(a), __float_as_uint(b), false, false);
  return __uint_as_float(s[0]) + __uint_as_float(s[1]);
}
__device__ __forceinline__ float swap32_add(float a, float b) {
  auto s = __builtin_amdgcn_permlane32_swap(__float_as_uint(a), __float_as_uint(b), false, false);
  return __uint_as_float(s[0]) + __uint_as_float(s[1]);
}
__device__ __forceinline__ float x16_max(float x) {
  auto s = __builtin_amdgcn_permlane16_swap(__float_as_uint(x), __float_as_uint(x), false, false);
  x = fmaxf(__uint_as_float(s[0]), __uint_as_float(s[1]));
  auto t = __builtin_amdgcn_permlane32_swap(__float_as_uint(x), __float_as_uint(x), false, false);
  return fmaxf(__uint_as_float(t[0]), __uint_as_float(t[1]));
}
__device__ __forceinline__ float wave_sum2(float x0, float x1) {
  float x = swap32_add(x0, x1);
  x += dpp<XOR1>(x); x += dpp<XOR2>(x); x += dpp<XOR7>(x); x += dpp<XOR8>(x);
  return swap16_add(x, x);
}

constexpr size_t CTL_STRIDE = 512, WS_REC = 65536;
constexpr int REC_B = 1152;

__device__ __forceinline__ u64 epoch_tag(u64 e) { return (e * 0x9E3779B97F4A7C15ull + 0xD1B54A32D192ED03ull) << 16; }

__device__ __forceinline__ void tag_counter(u64* w, u64 seen, u64 tag) {
  const u64 want = ((seen ^ tag) >> 16) == 0 ? seen : tag;
  u64 expect = seen;
  __hip_atomic_compare_exchange_strong(w, &expect, want, __ATOMIC_RELAXED, __ATOMIC_RELAXED, __HIP_MEMORY_SCOPE_AGENT);
}

constexpr int L_SC = 0, L_REC = WAVES * S * 64, L_ML = L_REC + WAVES * 256, L_END = 21 * 1024;
static_assert(L_ML + WAVES * 4 <= L_END, "");

__global__ void __launch_bounds__(NTHR, 1)
kv_decode(const unsigned short* __restrict__ q, const int* __restrict__ qk, const int* __restrict__ qv,
          const unsigned short* __restrict__ ks, const unsigned short* __restrict__ kz,
          const unsigned short* __restrict__ vs, const unsigned short* __restrict__ vz,
          unsigned char* __restrict__ ws, unsigned short* __restrict__ out) {
  __shared__ __attribute__((aligned(16))) float lds[L_END];
  const unsigned tstart = (unsigned)__builtin_amdgcn_s_memrealtime();
  const int qid = blockIdx.x / WGQ, wgq = blockIdx.x % WGQ;
  const int kvh = qid / NGK, grp = qid % NGK;
  const int tid = threadIdx.x, lane = tid & 63;
  const int wave = __builtin_amdgcn_readfirstlane(tid >> 6);
  const int s = lane & 7, r = lane >> 3;
  const int tbase = wgq * (NT / WGQ) + wave;
#define TILE(i_) (tbase + ((i_) & (S - 1)) * WAVES)
  unsigned pace;
  { const unsigned ph = ((qid * 280 + wgq * 21 + wave * 256) & 1023) * PACE_T / 1024;
    const unsigned t0 = tstart + PACE_0 - PACE_T / 2, m = (t0 - ph) % (unsigned)PACE_T;
    pace = t0 + (m ? PACE_T - m : 0); }
  float* sc = lds + L_SC + wave * (S * 64);
  unsigned char* ctl = ws + (size_t)kvh * CTL_STRIDE;
  u64* ep_p = reinterpret_cast<u64*>(ctl);
  u64* dn_p = reinterpret_cast<u64*>(ctl + 256);

  u64* ctl_p = reinterpret_cast<u64*>(ctl + (lane == 1 ? 256 : 0));
  const u64 pq = (u64)(q + (size_t)(kvh * 2) * DH), pks = (u64)(ks + (size_t)qid * DH) - 512, pkz = (u64)(kz + (size_t)qid * DH) - 768;
  u64 rowa = lane < 32 ? pq : pks;
  rowa = lane < 48 ? rowa : pkz;

  const size_t tok0 = (size_t)qid * GTOK;
  const int* kbase = qk + tok0 * DH;
  const int* vbase = qv + tok0 * DH;
  const int voff_ = r * 2048 + s * 16;
  int voff = voff_;
  i32x4 bA[16], bB[16];
#define ISSUE(buf, isk, tile) do { \
    const __amdgpu_buffer_rsrc_t rs_ = __builtin_amdgcn_make_buffer_rsrc((void*)((isk) ? kbase : vbase), 0, NT * TB, 0x00020000); \
    const int so_ = __builtin_amdgcn_readfirstlane((tile) * TB); \
    voff = voff_; asm volatile("" : "+v"(voff));     \
    __builtin_amdgcn_sched_barrier(0); \
    _Pragma("unroll") for (int j_ = 0; j_ < 16; ++j_) \
      buf[j_] = __builtin_bit_cast(i32x4, __builtin_amdgcn_raw_buffer_load_b128(rs_, voff + j_ * 128, so_, AUX_NT)); \
    __builtin_amdgcn_sched_barrier(0); } while (0)
  int calm = 0, skip = 0;
#define PACE() do { pace += PACE_T; \
    const int slack_ = (int)(pace - (unsigned)__builtin_amdgcn_s_memrealtime()); \
    if (skip > 0) --skip; \
    else if (slack_ < BO_LOW && calm >= 3) { pace += BO_SHIFT; skip = 1000; }     \
    else calm = slack_ >= BO_HIGH ? (calm < 6 ? calm + 1 : calm) : (calm > 2 ? calm - 2 : 0); \
    while ((int)((unsigned)__builtin_amdgcn_s_memrealtime() - pace) < 0) __builtin_amdgcn_s_sleep(1); } while (0)
#define WAVE_SYNC() do { __builtin_amdgcn_fence(__ATOMIC_ACQ_REL, "wavefront"); __builtin_amdgcn_wave_barrier(); } while (0)
  ISSUE(bA, true, TILE(0));
  u32x4 rowv = *reinterpret_cast<const __attribute__((address_space(1))) u32x4*>(rowa + lane * 16);
  u64 ctlv = 0;
  {
    const u64 ta = lane == 2 ? (u64)(vbase + (size_t)tbase * (TB / 4)) : (u64)ctl_p;
    if (wave == 0) ctlv = __hip_atomic_load(reinterpret_cast<__attribute__((address_space(1))) u64*>(ta), __ATOMIC_RELAXED, __HIP_MEMORY_SCOPE_AGENT);
  }
  ISSUE(bB, true, TILE(1));
  int cpf0 = 0, cpf1 = 0;
  {
    u64 pc; unsigned clen;
    asm volatile("s_getpc_b64 %0\nkvq_code_here:\n\ts_mov_b32 %1, kvq_code_end-kvq_code_here" : "=s"(pc), "=s"(clen));
    if (wave == 1) {
      const unsigned o0 = lane * 128, o1 = 8192 + lane * 128;
      cpf0 = *reinterpret_cast<const __attribute__((address_space(1))) int*>(pc + (o0 < clen ? o0 : 0));
      cpf1 = *reinterpret_cast<const __attribute__((address_space(1))) int*>(pc + (o1 < clen ? o1 : 0));
    }
  }
  u32x4 vsr[2], vzr[2];
#pragma unroll
  for (int i = 0; i < 2; ++i) {
    const size_t t = tok0 + (size_t)TILE((lane >> 2) + 16 * i) * TT + 8 * (lane & 3);
    vsr[i] = __builtin_nontemporal_load(reinterpret_cast<const u32x4*>(vs + t));
    vzr[i] = __builtin_nontemporal_load(reinterpret_cast<const u32x4*>(vz + t));
  }
  __builtin_amdgcn_sched_barrier(0);
  asm volatile("" : "+v"(ctlv), "+v"(rowv));
  const u64 ep = ((u64)(unsigned)__builtin_amdgcn_readfirstlane((int)(ctlv >> 32)) << 32) | (unsigned)__builtin_amdgcn_readfirstlane((int)ctlv);
  if (tid == 1) tag_counter(dn_p, ctlv, epoch_tag(ep));
  *reinterpret_cast<u32x4*>(sc + lane * 4) = rowv;
  WAVE_SYNC();

  constexpr float C2 = 0.088388347648318440f * 1.4426950408889634f;
  f32x2 a0[8], a1[8];
  f32x2 c0 = {0.f, 0.f}, c1 = {0.f, 0.f};
#pragma unroll
  for (int j = 0; j < 4; ++j) {
    const u32x2 q0v = *reinterpret_cast<const u32x2*>(sc + 16 * j + 2 * s), q1v = *reinterpret_cast<const u32x2*>(sc + 64 + 16 * j + 2 * s);
    const u32x2 ksv = *reinterpret_cast<const u32x2*>(sc + 128 + 16 * j + 2 * s), kzv = *reinterpret_cast<const u32x2*>(sc + 192 + 16 * j + 2 * s);
#pragma unroll
    for (int i = 0; i < 2; ++i) {
      const f32x2 fs = f32x2{bf_lo(ksv[i]), bf_hi(ksv[i])} * C2, fz = {bf_lo(kzv[i]), bf_hi(kzv[i])};
      const f32x2 w0 = f32x2{bf_lo(q0v[i]), bf_hi(q0v[i])} * fs, w1 = f32x2{bf_lo(q1v[i]), bf_hi(q1v[i])} * fs;
      a0[j * 2 + i] = w0; a1[j * 2 + i] = w1;
      c0 -= w0 * fz; c1 -= w1 * fz;
    }
  }
  WAVE_SYNC();

  const bool b3 = (lane & 8) != 0, b2 = (lane & 4) != 0, b1 = (lane & 2) != 0, b0 = (lane & 1) != 0;
#define KSTEP(buf, i_, NEXT) do { \
    float x[8]; \
    _Pragma("unroll") for (int g = 0; g < 4; ++g) { \
      f32x2 s0 = c0, s1 = c1; \
      _Pragma("unroll") for (int j = 0; j < 4; ++j) { \
        const i32x4 k4 = buf[g * 4 + j]; \
        const f32x2 xa = {(float)k4[0], (float)k4[1]}, xb = {(float)k4[2], (float)k4[3]}; \
        s0 += a0[j * 2] * xa; s1 += a1[j * 2] * xa; \
        s0 += a0[j * 2 + 1] * xb; s1 += a1[j * 2 + 1] * xb; } \
      x[2 * g] = s0.x + s0.y; x[2 * g + 1] = s1.x + s1.y; } \
      \
    _Pragma("unroll") for (int v = 0; v < 4; ++v) { const float keep = b2 ? x[v + 4] : x[v], send = b2 ? x[v] : x[v + 4]; x[v] = keep + dpp<XOR7>(send); } \
    _Pragma("unroll") for (int v = 0; v < 2; ++v) { const float keep = b1 ? x[v + 2] : x[v], send = b1 ? x[v] : x[v + 2]; x[v] = keep + dpp<XOR2>(send); } \
    { const float keep = b0 ? x[1] : x[0], send = b0 ? x[0] : x[1]; x[0] = keep + dpp<XOR1>(send); } \
    mx = fmaxf(mx, x[0]); \
    sc[(i_) * 64 + lane] = x[0]; \
    asm volatile("" : "+v"(mx)); \
    NEXT; } while (0)
  float mx = -INFINITY;
  for (int i = 0; i < S; i += 2) {
    const bool k = i + 2 < S;
    if (!k) skip = skip > 5 ? skip : 5;
    KSTEP(bA, i, PACE(); ISSUE(bA, k, TILE(i + 2)));
    KSTEP(bB, i + 1, PACE(); ISSUE(bB, k, TILE(i + 3)));
  }
#undef KSTEP

  asm volatile("" :: "v"(cpf0), "v"(cpf1));
  mx = fmaxf(mx, dpp<XOR2>(mx)); mx = fmaxf(mx, dpp<ROR4>(mx)); mx = fmaxf(mx, dpp<XOR8>(mx));
  mx = x16_max(mx);
  const float mo = dpp<XOR1>(mx);
  const float m0 = b0 ? mo : mx, m1 = b0 ? mx : mo;
  float l0 = 0.f, l1 = 0.f, z0 = 0.f, z1 = 0.f;
#pragma unroll
  for (int i = 0; i < 2; ++i) asm volatile("" : "+v"(vsr[i]), "+v"(vzr[i]));
  WAVE_SYNC();
#pragma unroll
  for (int i = 0; i < 2; ++i) {
    float* p = sc + (lane + 64 * i) * 16;
#pragma unroll
    for (int e = 0; e < 4; ++e) {
      const f32x4 sv = *reinterpret_cast<const f32x4*>(p + 4 * e);
      const float pa0 = __builtin_amdgcn_exp2f(sv[0] - m0), pa1 = __builtin_amdgcn_exp2f(sv[1] - m1);
      const float pb0 = __builtin_amdgcn_exp2f(sv[2] - m0), pb1 = __builtin_amdgcn_exp2f(sv[3] - m1);
      l0 += pa0 + pb0; l1 += pa1 + pb1;
      const float sa = bf_lo(vsr[i][e]), sb = bf_hi(vsr[i][e]), za = bf_lo(vzr[i][e]), zb = bf_hi(vzr[i][e]);
      const f32x4 w = {pa0 * sa, pa1 * sa, pb0 * sb, pb1 * sb};
      z0 = fmaf(w[0], za, z0); z1 = fmaf(w[1], za, z1);
      z0 = fmaf(w[2], zb, z0); z1 = fmaf(w[3], zb, z1);
      *reinterpret_cast<f32x4*>(p + 4 * e) = w;
    }
  }
  WAVE_SYNC();
  const float zs = wave_sum2(z0, z1);
  if ((lane & 31) == 0) lds[L_ML + wave * 4 + (lane >> 4)] = lane ? m1 : m0;
  { const float lsum = wave_sum2(l0, l1); if ((lane & 31) == 0) lds[L_ML + wave * 4 + (lane >> 4) + 1] = lsum; }

  f32x2 acc0[8], acc1[8];
#pragma unroll
  for (int i = 0; i < 8; ++i) { acc0[i] = f32x2{0.f, 0.f}; acc1[i] = f32x2{0.f, 0.f}; }
#define VSTEP(buf, i_, NEXT) do { \
    const f32x4 wa = *reinterpret_cast<const f32x4*>(sc + (i_) * 64 + 8 * r), wb = *reinterpret_cast<const f32x4*>(sc + (i_) * 64 + 8 * r + 4); \
    _Pragma("unroll") for (int g = 0; g < 4; ++g) { \
      const float w0 = g < 2 ? wa[2 * g] : wb[2 * g - 4], w1 = g < 2 ? wa[2 * g + 1] : wb[2 * g - 3]; \
      _Pragma("unroll") for (int j = 0; j < 4; ++j) { \
        const i32x4 v4 = buf[g * 4 + j]; \
        const f32x2 xa = {(float)v4[0], (float)v4[1]}, xb = {(float)v4[2], (float)v4[3]}; \
        acc0[j * 2] += w0 * xa; acc1[j * 2] += w1 * xa; \
        acc0[j * 2 + 1] += w0 * xb; acc1[j * 2 + 1] += w1 * xb; } } \
    _Pragma("unroll") for (int i = 0; i < 8; ++i) asm volatile("" : "+v"(acc0[i]), "+v"(acc1[i])); \
    NEXT; } while (0)
  for (int i = 0; i < S - 2; i += 2) {
    VSTEP(bA, i, PACE(); ISSUE(bA, false, TILE(i + 2)));
    VSTEP(bB, i + 1, PACE(); ISSUE(bB, false, TILE(i + 3)));
  }
  VSTEP(bA, S - 2, (void)0);
  VSTEP(bB, S - 1, (void)0);
#undef VSTEP
#undef ISSUE
#undef PACE
#undef TILE
  {
    float y[16];
#pragma unroll
    for (int i = 0; i < 8; ++i) { y[2 * i] = swap32_add(acc0[i].x, acc1[i].x); y[2 * i + 1] = swap32_add(acc0[i].y, acc1[i].y); }
#pragma unroll
    for (int i = 0; i < 8; ++i) y[i] = swap16_add(y[i], y[i + 8]);
#pragma unroll
    for (int i = 0; i < 4; ++i) { const float keep = b3 ? y[i + 4] : y[i], send = b3 ? y[i] : y[i + 4]; y[i] = keep + dpp<XOR8>(send); }
    *reinterpret_cast<f32x4*>(&lds[L_REC + wave * 256 + lane * 4]) = f32x4{y[0] - zs, y[1] - zs, y[2] - zs, y[3] - zs};
  }
  __syncthreads();
  if (wave != 0) return;

  const int h = lane >> 5;
  const __amdgpu_buffer_rsrc_t rp = __builtin_amdgcn_make_buffer_rsrc((void*)(ws + WS_REC + (size_t)kvh * NREC * REC_B), 0, NREC * REC_B, 0x00020000);
  {
    float M = -INFINITY;
#pragma unroll
    for (int w = 0; w < WAVES; ++w) M = fmaxf(M, lds[L_ML + w * 4 + 2 * h]);
    float L = 0.f;
    f32x4 o = {0.f, 0.f, 0.f, 0.f};
#pragma unroll
    for (int w = 0; w < WAVES; ++w) {
      const float wt = __builtin_amdgcn_exp2f(lds[L_ML + w * 4 + 2 * h] - M);
      L = fmaf(wt, lds[L_ML + w * 4 + 2 * h + 1], L);
      o += wt * *reinterpret_cast<const f32x4*>(&lds[L_REC + w * 256 + lane * 4]);
    }
    const int rec = (grp * WGQ + wgq) * REC_B;
    __builtin_amdgcn_raw_buffer_store_b128(__builtin_bit_cast(u32x4, o), rp, lane * 16, rec, AUX_SC1);
    if ((lane & 31) == 0)
      __builtin_amdgcn_raw_buffer_store_b64(__builtin_bit_cast(u32x2, f32x2{M, L}), rp, 1024 + h * 8, rec, AUX_SC1);
  }
  asm volatile("s_waitcnt vmcnt(0)" ::: "memory");
  unsigned old = 0;
  if (lane == 0) old = __hip_atomic_fetch_add(reinterpret_cast<unsigned*>(dn_p), 1u, __ATOMIC_RELAXED, __HIP_MEMORY_SCOPE_AGENT);
  if ((__builtin_amdgcn_readfirstlane(old) & 0xffffu) != NREC - 1) return;
  __builtin_amdgcn_fence(__ATOMIC_ACQUIRE, "wavefront");

  {
    f32x4 ov[NREC];
    f32x2 ml[NREC];
#pragma unroll
    for (int p = 0; p < NREC; ++p) {
      ml[p] = __builtin_bit_cast(f32x2, __builtin_amdgcn_raw_buffer_load_b64(rp, 1024 + h * 8, p * REC_B, AUX_SC1));
      ov[p] = __builtin_bit_cast(f32x4, __builtin_amdgcn_raw_buffer_load_b128(rp, lane * 16, p * REC_B, AUX_SC1));
    }
    float M = -INFINITY;
#pragma unroll
    for (int p = 0; p < NREC; ++p) M = fmaxf(M, ml[p].x);
    float L = 0.f;
    f32x4 o = {0.f, 0.f, 0.f, 0.f};
#pragma unroll
    for (int p = 0; p < NREC; ++p) {
      const float wt = __builtin_amdgcn_exp2f(ml[p].x - M);
      L = fmaf(wt, ml[p].y, L);
      o += wt * ov[p];
    }
    const float inv = 1.0f / L;
    unsigned short b[4];
#pragma unroll
    for (int e = 0; e < 4; ++e) b[e] = __builtin_bit_cast(unsigned short, __float2bfloat16(o[e] * inv));
    const u32x2 pk = {(unsigned)b[0] | ((unsigned)b[1] << 16), (unsigned)b[2] | ((unsigned)b[3] << 16)};
    *reinterpret_cast<u32x2*>(out + (size_t)kvh * 2 * DH + lane * 4) = pk;
    asm volatile("kvq_code_end:");
    if (lane == 0) __hip_atomic_store(ep_p, ep + 1ull, __ATOMIC_RELAXED, __HIP_MEMORY_SCOPE_AGENT);
  }
}

}

extern "C" void kernel_launch(void* const* d_in, const int* in_sizes, int n_in, void* d_out, int out_size,
                              void* d_ws, size_t ws_size, hipStream_t stream) {
  using namespace kvq;
  kv_decode<<<dim3(NWG), dim3(NTHR), 0, stream>>>(
      (const unsigned short*)d_in[0], (const int*)d_in[1], (const int*)d_in[2], (const unsigned short*)d_in[3],
      (const unsigned short*)d_in[4], (const unsigned short*)d_in[5], (const unsigned short*)d_in[6],
      (unsigned char*)d_ws, (unsigned short*)d_out);
}
```
